```python
import torch

def _softmax(x, axis=-1):
    return torch.softmax(x, dim=axis)

def _gelu(x):
    return torch.nn.functional.gelu(x, approximate='tanh')

def _silu(x):
    return torch.nn.functional.silu(x)

def _sigmoid(x):
    return torch.sigmoid(x)

def _layernorm(x, axis=-1, eps=1e-05):
    m = x.mean(dim=axis, keepdim=True)
    v = x.var(dim=axis, keepdim=True, unbiased=False)
    return (x - m) / torch.sqrt(v + eps)

def _scatter_add(tgt, idx, src, axis=0):
    return tgt.clone().index_add_(axis, idx, src)

def _segment_sum(src, seg, n):
    out = torch.zeros((n,) + src.shape[1:], dtype=src.dtype)
    return out.index_add_(0, seg, src)

def _pad(x, pw):
    flat = tuple((v for pair in reversed(pw) for v in pair))
    return torch.nn.functional.pad(x, flat)
kernel_name = 'standalone__tree_speculative_verify_tree_attention_over_candidate_token_tree__decode'

def setup_inputs():

    _w = lambda a: (lambda t: t.to(torch.bfloat16) if t.is_floating_point() else t)(torch.as_tensor(a))
    B, Hq, Hkv, T, C, D = (1, 32, 16, 1, 32768, 128)
    tree_mask = torch.zeros((B, T, T), dtype=float)
    for b in range(B):
        parent = torch.zeros(T, dtype=torch.int64)
        for i in range(1, T):
            parent[i] = int(torch.randint(0, i, (1,)).item())
        for i in range(T):
            j = i
            tree_mask[b, i, j] = 1.0
            while j > 0:
                j = parent[j]
                tree_mask[b, i, j] = 1.0
    return {'q': _w((lambda *s: torch.randn(*s, dtype=torch.bfloat16) * 0.8)(B, Hq, T, D)), 'k_cache': _w((lambda *s: torch.randn(*s, dtype=torch.bfloat16) * 0.8)(B, Hkv, C, D)), 'v_cache': _w((lambda *s: torch.randn(*s, dtype=torch.bfloat16) * 0.05)(B, Hkv, C, D)), 'k_tree': _w((lambda *s: torch.randn(*s, dtype=torch.bfloat16) * 0.8)(B, Hkv, T, D)), 'v_tree': _w((lambda *s: torch.randn(*s, dtype=torch.bfloat16) * 0.05)(B, Hkv, T, D)), 'tree_mask': _w(tree_mask)}

def reference(q, k_cache, v_cache, k_tree, v_tree, tree_mask):
    B, Hq, T, D = q.shape
    Hkv, C = (k_cache.shape[1], k_cache.shape[2])
    rep = Hq // Hkv
    kc = (lambda x, n, axis: torch.repeat_interleave(x, n, dim=axis))(k_cache, rep, axis=1)
    vc = (lambda x, n, axis: torch.repeat_interleave(x, n, dim=axis))(v_cache, rep, axis=1)
    kt = (lambda x, n, axis: torch.repeat_interleave(x, n, dim=axis))(k_tree, rep, axis=1)
    vt = (lambda x, n, axis: torch.repeat_interleave(x, n, dim=axis))(v_tree, rep, axis=1)
    k = torch.concatenate([kc, kt], axis=2)
    v = torch.concatenate([vc, vt], axis=2)
    scale = D ** (-0.5)
    scores = torch.einsum('bhqd,bhkd->bhqk', q, k) * scale
    ctx_mask = torch.ones((B, 1, T, C), dtype=scores.dtype)
    tm = tree_mask[:, None, :, :].clone()
    full_mask = torch.concatenate([ctx_mask, tm], axis=-1)
    scores = scores + (1.0 - full_mask) * -1000000000.0
    p = _softmax(scores, axis=-1)
    return torch.einsum('bhqk,bhkd->bhqd', p, v)
```

```cpp
#include <hip/hip_runtime.h>
#include <hip/hip_bf16.h>
#include <stdint.h>
#include <type_traits>

typedef unsigned u32x4 __attribute__((ext_vector_type(4)));
typedef float f32x2 __attribute__((ext_vector_type(2)));
typedef float f32x4 __attribute__((ext_vector_type(4)));
typedef __bf16 bf16x2 __attribute__((ext_vector_type(2)));

constexpr int HQ = 32, HKV = 16, CTX = 32768, D = 128;
constexpr int NSPLIT = 16;
constexpr int NW = 8;
constexpr int NTHR = NW * 64;
constexpr int TILE = 16;
constexpr int NBUF = 4;
constexpr int TOK_WG = CTX / NSPLIT;
constexpr int NT_W = TOK_WG / NW / TILE;
static_assert(NT_W % NBUF == 0, "ring");
constexpr float SCALE = 0.088388347648318440f;
constexpr float LOG2E = 1.4426950408889634f;
constexpr float C2 = SCALE * LOG2E;
constexpr int REC = 2 * D + 4;

__device__ __forceinline__ float dot2(unsigned k, unsigned q, float acc) {
  return __builtin_amdgcn_fdot2_f32_bf16(__builtin_bit_cast(bf16x2, k), __builtin_bit_cast(bf16x2, q), acc, false);
}
template <int CTRL> __device__ __forceinline__ float dpp(float x) {
  return __builtin_bit_cast(float, __builtin_amdgcn_mov_dpp(__builtin_bit_cast(int, x), CTRL, 0xf, 0xf, true));
}
constexpr int XOR1 = 0xB1, XOR2 = 0x4E, XOR7 = 0x141, XOR8 = 0x128, ROR4 = 0x124, BCAST = 0x150;
__device__ __forceinline__ float bflo(unsigned x) { return __builtin_bit_cast(float, x << 16); }
__device__ __forceinline__ float bfhi(unsigned x) { return __builtin_bit_cast(float, x & 0xffff0000u); }
__device__ __forceinline__ float xrow_sum(float x) {
  auto s = __builtin_amdgcn_permlane16_swap(__float_as_uint(x), __float_as_uint(x), false, false);
  x = __uint_as_float(s[0]) + __uint_as_float(s[1]);
  auto t = __builtin_amdgcn_permlane32_swap(__float_as_uint(x), __float_as_uint(x), false, false);
  return __uint_as_float(t[0]) + __uint_as_float(t[1]);
}
__device__ __forceinline__ float xrow_max(float x) {
  auto s = __builtin_amdgcn_permlane16_swap(__float_as_uint(x), __float_as_uint(x), false, false);
  x = fmaxf(__uint_as_float(s[0]), __uint_as_float(s[1]));
  auto t = __builtin_amdgcn_permlane32_swap(__float_as_uint(x), __float_as_uint(x), false, false);
  return fmaxf(__uint_as_float(t[0]), __uint_as_float(t[1]));
}

__global__ void __launch_bounds__(NTHR, 2)
attn_split(const unsigned short* __restrict__ q, const unsigned short* __restrict__ kc, const unsigned short* __restrict__ vc,
           float* __restrict__ part) {
  __shared__ __attribute__((aligned(16))) float lds[NW * REC];
  const int wg = blockIdx.x, h = wg / NSPLIT, s = wg % NSPLIT;
  const int tid = threadIdx.x, lane = tid & 63, wave = __builtin_amdgcn_readfirstlane(tid >> 6), r = lane >> 4, c = lane & 15;
  const size_t base = ((size_t)h * CTX + (size_t)s * TOK_WG) * D;
  auto rk = __builtin_amdgcn_make_buffer_rsrc((void*)(kc + base), 0, TOK_WG * D * 2, 0x00020000);
  auto rv = __builtin_amdgcn_make_buffer_rsrc((void*)(vc + base), 0, TOK_WG * D * 2, 0x00020000);
  const unsigned voff = lane * 16;
  u32x4 Kb[NBUF][4], Vb[NBUF][4];
  const u32x4 qa = *reinterpret_cast<const u32x4*>(q + (2 * h + (c & 1)) * D + 8 * c);
  const u32x4 qb = *reinterpret_cast<const u32x4*>(q + (2 * h + 1 - (c & 1)) * D + 8 * c);
#define LDK(b, t) _Pragma("unroll") for (int i_ = 0; i_ < 4; ++i_) Kb[b][i_] = __builtin_amdgcn_raw_buffer_load_b128(rk, voff, (((t) * NW + wave) * 4 + i_) * 1024, 2)
#define LDV(b, t) _Pragma("unroll") for (int i_ = 0; i_ < 4; ++i_) Vb[b][i_] = __builtin_amdgcn_raw_buffer_load_b128(rv, voff, (((t) * NW + wave) * 4 + i_) * 1024, 2)
#pragma unroll
  for (int b = 0; b < NBUF; ++b) { LDK(b, b); LDV(b, b); }
  __builtin_amdgcn_sched_barrier(0);
  float m = -INFINITY, l = 0.f;
  f32x2 acc[8];
#pragma unroll
  for (int k = 0; k < 8; ++k) acc[k] = f32x2{0.f, 0.f};

  auto pass = [&](auto more_c, int t0) __attribute__((always_inline)) {
    constexpr bool more = decltype(more_c)::value;
#pragma unroll
    for (int b = 0; b < NBUF; ++b) {
      float y[4];
#pragma unroll
      for (int i = 0; i < 4; ++i) {
        float a = 0.f, bb = 0.f;
#pragma unroll
        for (int k = 0; k < 4; ++k) { a = dot2(Kb[b][i][k], qa[k], a); bb = dot2(Kb[b][i][k], qb[k], bb); }
        y[i] = a + dpp<XOR1>(bb);
      }
      if constexpr (more) { LDK(b, t0 + NBUF + b); }
      __builtin_amdgcn_sched_barrier(0);
      float z0, z1;
      { const float t1 = y[0] + dpp<XOR8>(y[0]), t2 = y[2] + dpp<XOR8>(y[2]); z0 = (c & 8) ? t2 : t1; }
      { const float t1 = y[1] + dpp<XOR8>(y[1]), t2 = y[3] + dpp<XOR8>(y[3]); z1 = (c & 8) ? t2 : t1; }
      float sc;
      { const float t1 = z0 + dpp<XOR2>(z0), t2 = z1 + dpp<XOR2>(z1); sc = (c & 2) ? t2 : t1; }
      {
        const float up = dpp<0x104>(sc), dn = dpp<0x114>(sc);
        sc = sc + ((c & 4) ? dn : up);
      }
      sc *= C2;
      float pm = fmaxf(sc, dpp<XOR2>(sc)); pm = fmaxf(pm, dpp<XOR8>(pm));
      const float mn = fmaxf(m, pm);
      const float alpha = __builtin_amdgcn_exp2f(m - mn), p = __builtin_amdgcn_exp2f(sc - mn);
      l = fmaf(l, alpha, p); m = mn;
      const f32x2 al = {dpp<BCAST + 0>(alpha), dpp<BCAST + 1>(alpha)};
#pragma unroll
      for (int k = 0; k < 8; ++k) acc[k] *= al;
      f32x2 pp[4];
      pp[0] = f32x2{dpp<BCAST + 0>(p), dpp<BCAST + 1>(p)};
      pp[1] = f32x2{dpp<BCAST + 2>(p), dpp<BCAST + 3>(p)};
      pp[2] = f32x2{dpp<BCAST + 8>(p), dpp<BCAST + 9>(p)};
      pp[3] = f32x2{dpp<BCAST + 10>(p), dpp<BCAST + 11>(p)};
#pragma unroll
      for (int i = 0; i < 4; ++i) {
#pragma unroll
        for (int k = 0; k < 4; ++k) {
          const float lo = bflo(Vb[b][i][k]), hi = bfhi(Vb[b][i][k]);
          acc[2 * k] = __builtin_elementwise_fma(pp[i], f32x2{lo, lo}, acc[2 * k]);
          acc[2 * k + 1] = __builtin_elementwise_fma(pp[i], f32x2{hi, hi}, acc[2 * k + 1]);
        }
      }
      if constexpr (more) { LDV(b, t0 + NBUF + b); }
      __builtin_amdgcn_sched_barrier(0);
    }
  };
#pragma unroll 1
  for (int t0 = 0; t0 < NT_W - NBUF; t0 += NBUF) pass(std::true_type{}, t0);
  pass(std::false_type{}, 0);
  { float t = l + dpp<XOR2>(l); l = t + dpp<XOR8>(t); }
  const float M = xrow_max(m);
  const float w = __builtin_amdgcn_exp2f(m - M);
  l = xrow_sum(l * w);
  const f32x2 w2 = {dpp<BCAST + 0>(w), dpp<BCAST + 1>(w)};
  float o0[8], o1[8];
#pragma unroll
  for (int k = 0; k < 8; ++k) { const f32x2 t = acc[k] * w2; o0[k] = xrow_sum(t[0]); o1[k] = xrow_sum(t[1]); }
  float* rec = lds + wave * REC;
  if (r == 0) { *reinterpret_cast<f32x4*>(rec + 4 + 8 * c) = f32x4{o0[0], o0[1], o0[2], o0[3]}; *reinterpret_cast<f32x4*>(rec + 8 + 8 * c) = f32x4{o0[4], o0[5], o0[6], o0[7]}; }
  if (r == 1) { *reinterpret_cast<f32x4*>(rec + 4 + D + 8 * c) = f32x4{o1[0], o1[1], o1[2], o1[3]}; *reinterpret_cast<f32x4*>(rec + 8 + D + 8 * c) = f32x4{o1[4], o1[5], o1[6], o1[7]}; }
  if (lane < 2) { rec[2 * lane] = M; rec[2 * lane + 1] = l; }
  __syncthreads();
  if (tid < 2 * D) {
    const int g = tid >> 7;
    float MM = -INFINITY;
#pragma unroll
    for (int ww = 0; ww < NW; ++ww) MM = fmaxf(MM, lds[ww * REC + 2 * g]);
    float L = 0.f, o = 0.f;
#pragma unroll
    for (int ww = 0; ww < NW; ++ww) { const float wt = __builtin_amdgcn_exp2f(lds[ww * REC + 2 * g] - MM); L = fmaf(wt, lds[ww * REC + 2 * g + 1], L); o = fmaf(wt, lds[ww * REC + 4 + tid], o); }
    float* pr = part + (size_t)wg * REC;
    pr[4 + tid] = o;
    if ((tid & 127) == 0) { pr[2 * g] = MM; pr[2 * g + 1] = L; }
  }
}

__global__ void __launch_bounds__(128)
attn_combine(const float* __restrict__ part, const unsigned short* __restrict__ q, const unsigned short* __restrict__ kt,
             const unsigned short* __restrict__ vt, const unsigned short* __restrict__ mask, __hip_bfloat16* __restrict__ out) {
  __shared__ float red[2];
  const int hq = blockIdx.x, h = hq >> 1, g = hq & 1, d = threadIdx.x;
  float ms[NSPLIT], ls[NSPLIT], os[NSPLIT];
#pragma unroll
  for (int s = 0; s < NSPLIT; ++s) { const float* pr = part + (size_t)(h * NSPLIT + s) * REC; ms[s] = pr[2 * g]; ls[s] = pr[2 * g + 1]; os[s] = pr[4 + g * D + d]; }
  float x = bflo(q[hq * D + d]) * bflo(kt[h * D + d]);
  const float vtd = bflo(vt[h * D + d]), mk = bflo(mask[0]);
#pragma unroll
  for (int o = 32; o > 0; o >>= 1) x += __shfl_xor(x, o);
  if ((d & 63) == 0) red[d >> 6] = x;
  __syncthreads();
  const float st = ((red[0] + red[1]) * SCALE + (1.0f - mk) * -1000000000.0f) * LOG2E;
  float M = st;
#pragma unroll
  for (int s = 0; s < NSPLIT; ++s) M = fmaxf(M, ms[s]);
  const float pt = __builtin_amdgcn_exp2f(st - M);
  float L = pt, o = pt * vtd;
#pragma unroll
  for (int s = 0; s < NSPLIT; ++s) { const float wt = __builtin_amdgcn_exp2f(ms[s] - M); L = fmaf(wt, ls[s], L); o = fmaf(wt, os[s], o); }
  out[hq * D + d] = __float2bfloat16(o / L);
}

extern "C" void kernel_launch(void* const* d_in, const int* in_sizes, int n_in,
                              void* d_out, int out_size, void* d_ws, size_t ws_size,
                              hipStream_t stream) {
  const unsigned short* q = (const unsigned short*)d_in[0];
  const unsigned short* kc = (const unsigned short*)d_in[1];
  const unsigned short* vc = (const unsigned short*)d_in[2];
  const unsigned short* kt = (const unsigned short*)d_in[3];
  const unsigned short* vt = (const unsigned short*)d_in[4];
  const unsigned short* mask = (const unsigned short*)d_in[5];
  float* part = (float*)d_ws;
  attn_split<<<HKV * NSPLIT, NTHR, 0, stream>>>(q, kc, vc, part);
  attn_combine<<<HQ, 128, 0, stream>>>(part, q, kt, vt, mask, (__hip_bfloat16*)d_out);
}
```

```cpp
#include <hip/hip_runtime.h>
#include <hip/hip_bf16.h>
#include <stdint.h>

typedef unsigned u32x2 __attribute__((ext_vector_type(2)));
typedef unsigned u32x4 __attribute__((ext_vector_type(4)));
typedef float f32x2 __attribute__((ext_vector_type(2)));
typedef float f32x4 __attribute__((ext_vector_type(4)));
typedef __bf16 bf16x2 __attribute__((ext_vector_type(2)));
typedef unsigned long long u64;
typedef __attribute__((address_space(1))) u64 gu64;
#define LAS __attribute__((address_space(3)))
#define RLX_AGENT __ATOMIC_RELAXED, __HIP_MEMORY_SCOPE_AGENT
#define FENCE() __builtin_amdgcn_sched_barrier(0)

constexpr int HKV = 16, CTX = 32768, D = 128;
constexpr int NSPLIT = 16;
constexpr int NW = 4;
constexpr int NTHR = (NW + 1) * 64;
constexpr int NT = CTX / NSPLIT / NW / 32;
constexpr int QS = 4;
constexpr unsigned HEAD_BYTES = CTX * D * 2;
constexpr unsigned PIECE = QS * NW * 4096u;
constexpr unsigned RSRC = 0x00020000;
constexpr int AUX_NT = 2, AUX_SC1 = 16;
constexpr float SCALE = 0.088388347648318440f;
constexpr float LOG2E = 1.4426950408889634f;
constexpr int WIN = 7;
constexpr int WT = 5, PRO = 4;
constexpr unsigned TS = 200, TE = 3525;
constexpr int SPRINT = 2;
constexpr unsigned SPIN_CAP = 1u << 16;
constexpr int WS_IN = 0, WS_X = HKV * NSPLIT * NSPLIT * 16;
constexpr int L_O = 0, L_ML = NW * 2 * D, L_TAG = L_ML + 4 * NW, L_REF = L_TAG + NSPLIT, L_S = L_REF + 4, L_END = L_S + NW * NT * 64;

__device__ __forceinline__ float dot2(unsigned k, unsigned q, float acc) {
  return __builtin_amdgcn_fdot2_f32_bf16(__builtin_bit_cast(bf16x2, k), __builtin_bit_cast(bf16x2, q), acc, false);
}
template <int CTRL> __device__ __forceinline__ int idpp(int x) { return __builtin_amdgcn_mov_dpp(x, CTRL, 0xf, 0xf, true); }
template <int CTRL> __device__ __forceinline__ float dpp(float x) { return __builtin_bit_cast(float, idpp<CTRL>(__builtin_bit_cast(int, x))); }
constexpr int XOR1 = 0xB1, XOR2 = 0x4E, XOR7 = 0x141, XOR8 = 0x128, ROR4 = 0x124, SHL4 = 0x104, SHR4 = 0x114, BCAST = 0x150;
__device__ __forceinline__ float bflo(unsigned x) { return __builtin_bit_cast(float, x << 16); }
__device__ __forceinline__ float bfhi(unsigned x) { return __builtin_bit_cast(float, x & 0xffff0000u); }
__device__ __forceinline__ float rs32(float a, float b) {
  auto t = __builtin_amdgcn_permlane32_swap(__float_as_uint(a), __float_as_uint(b), false, false);
  return __uint_as_float(t[0]) + __uint_as_float(t[1]);
}
__device__ __forceinline__ float rs16(float a, float b) {
  auto t = __builtin_amdgcn_permlane16_swap(__float_as_uint(a), __float_as_uint(b), false, false);
  return __uint_as_float(t[0]) + __uint_as_float(t[1]);
}
__device__ __forceinline__ float xrow_sum(float x) { x = rs16(x, x); return rs32(x, x); }
__device__ __forceinline__ float xrow_max(float x) {
  auto s = __builtin_amdgcn_permlane16_swap(__float_as_uint(x), __float_as_uint(x), false, false);
  x = fmaxf(__uint_as_float(s[0]), __uint_as_float(s[1]));
  auto t = __builtin_amdgcn_permlane32_swap(__float_as_uint(x), __float_as_uint(x), false, false);
  return fmaxf(__uint_as_float(t[0]), __uint_as_float(t[1]));
}
__device__ __forceinline__ u64 pack(unsigned lo, unsigned hi) { return ((u64)hi << 32) | lo; }

__global__ void __launch_bounds__(NTHR, 2)
attn_fused(const unsigned short* __restrict__ q, const unsigned short* __restrict__ kc, const unsigned short* __restrict__ vc,
           const unsigned short* __restrict__ kt, const unsigned short* __restrict__ vt, const unsigned short* __restrict__ mask,
           unsigned short* __restrict__ out, u64* ws) {
  __shared__ __attribute__((aligned(16))) float lds[L_END];
  volatile LAS unsigned* const lds_u = (volatile LAS unsigned*)lds;
  asm volatile("" :: "s"(q), "s"(kc), "s"(vc), "s"(kt), "s"(vt), "s"(mask), "s"(out), "s"(ws));
  const int wg = blockIdx.x, s = wg >> 4, h = (wg - s) & 15;
  const int tid = threadIdx.x, lane = tid & 63, wave = __builtin_amdgcn_readfirstlane(tid >> 6), r = lane >> 4, c = lane & 15;
  gu64* const inbox = (gu64*)ws + WS_IN + h * (NSPLIT * NSPLIT * 16);

  if (wave == NW) {
    const unsigned t_own = (unsigned)__builtin_amdgcn_s_memrealtime();
    gu64* const xin = (gu64*)ws + WS_X + (h * NSPLIT + c) * NSPLIT + s;
    gu64* const xout = (gu64*)ws + WS_X + (h * NSPLIT + s) * NSPLIT + c;
    gu64* const mybox = inbox + s * 256 + lane;
    u64 x = __hip_atomic_load(xin, RLX_AGENT);
    u32x2 found[4];
#pragma unroll
    for (int i = 0; i < 4; ++i)
      found[i] = __builtin_amdgcn_raw_buffer_load_b64(__builtin_amdgcn_make_buffer_rsrc((void*)(inbox + s * 256), 0, 2048, RSRC),
                                                      lane * 8 + i * 512, 0, AUX_SC1 | AUX_NT);
    lds_u[L_REF] = t_own;
    asm volatile("s_waitcnt lgkmcnt(0)\n\ts_barrier" ::: "memory");
    const unsigned qq = *reinterpret_cast<const unsigned*>(q + (2 * h + (s >> 3)) * D + 2 * lane);
    const unsigned kk = *reinterpret_cast<const unsigned*>(kt + h * D + 2 * lane);
    float tree_v = bflo(vt[h * D + (s & 7) * 16 + c]);
    const float mk = bflo(mask[0]);
    unsigned want[4], mytag = 0;
#pragma unroll
    for (int i = 0; i < 4; ++i) {
      int used = 1 << (found[i][0] & 31u);
      used |= idpp<XOR1>(used); used |= idpp<XOR2>(used); used |= idpp<XOR7>(used); used |= idpp<XOR8>(used);
      want[i] = __builtin_ctz(~used);
      const unsigned t = __shfl(want[i], (c & 3) * 16);
      mytag = (c >> 2) == i ? t : mytag;
    }
    unsigned mine = (t_own << 5) | mytag;
    mine += (mine == (unsigned)(x >> 32)) ? 32u : 0u;
    unsigned echoed = (unsigned)x;
    if (lane < 16) __hip_atomic_store(xout, pack(mine, echoed), RLX_AGENT);
    float d = dot2(qq, kk, 0.f);
    d += dpp<XOR1>(d); d += dpp<XOR2>(d); d += dpp<XOR7>(d); d += dpp<XOR8>(d);
    d = xrow_sum(d);
    float tree_s = (d * SCALE + (1.0f - mk) * -1000000000.0f) * LOG2E;
    bool ok = false;
    for (unsigned spin = 0; spin < SPIN_CAP && !ok; ++spin) {
      __builtin_amdgcn_s_sleep(127);
      x = __hip_atomic_load(xin, RLX_AGENT);
      if (__any((unsigned)x != echoed)) { echoed = (unsigned)x; if (lane < 16) __hip_atomic_store(xout, pack(mine, echoed), RLX_AGENT); }
      ok = __all((unsigned)(x >> 32) == mine);
    }
    {
      int dt = min(max((int)(echoed - (t_own << 5)) >> 5, -1000), 1000);
      dt += idpp<XOR1>(dt); dt += idpp<XOR2>(dt); dt += idpp<XOR7>(dt); dt += idpp<XOR8>(dt);
      lds_u[L_REF] = t_own + (dt >> 4);
      lds_u[L_TAG + c] = echoed & 31u;
    }
    unsigned oi = (2 * h + (s >> 3)) * D + (s & 7) * 16 + c;
    asm volatile("" : "+v"(tree_s), "+v"(tree_v), "+v"(oi));
    __syncthreads();
    __builtin_amdgcn_s_sleep(20);
    u64 g[4] = {0, 0, 0, 0};
    bool got = false;
    for (unsigned spin = 0; spin < 16 * SPIN_CAP && ok && !got; ++spin) {
#pragma unroll
      for (int i = 0; i < 4; ++i) g[i] = __hip_atomic_load(mybox + i * 64, RLX_AGENT);
      got = __all(((((unsigned)g[0] ^ want[0]) | ((unsigned)g[1] ^ want[1]) | ((unsigned)g[2] ^ want[2]) | ((unsigned)g[3] ^ want[3])) & 31u) == 0u);
    }
    float ls[4], ov[4];
#pragma unroll
    for (int i = 0; i < 4; ++i) { ls[i] = __uint_as_float((unsigned)(g[i] >> 32)); ov[i] = __uint_as_float((unsigned)g[i] & ~31u); }
    const float M = fmaxf(xrow_max(fmaxf(fmaxf(ls[0], ls[1]), fmaxf(ls[2], ls[3]))), tree_s);
    const float pt = __builtin_amdgcn_exp2f(tree_s - M);
    float L = 0.f, o = 0.f;
#pragma unroll
    for (int i = 0; i < 4; ++i) { const float wt = __builtin_amdgcn_exp2f(ls[i] - M); L += wt; o = fmaf(wt, ov[i], o); }
    L = xrow_sum(L); o = xrow_sum(o);
    float res = fmaf(pt, tree_v, o) * __builtin_amdgcn_rcpf(L + pt);
    if (!got) res = __uint_as_float(0x7fc00000u);
    if (lane < 16) out[oi] = __builtin_bit_cast(unsigned short, (__bf16)res);
  } else {
    const unsigned short* kb = kc + (size_t)h * CTX * D;
    const unsigned short* vb = vc + (size_t)h * CTX * D;
    const unsigned voff = lane * 16;
    const u32x4 qa = *reinterpret_cast<const u32x4*>(q + (2 * h + (c & 1)) * D + 8 * c);
    const u32x4 qb = *reinterpret_cast<const u32x4*>(q + (2 * h + 1 - (c & 1)) * D + 8 * c);
    FENCE();
    u32x4 B[8];
    const auto rk = __builtin_amdgcn_make_buffer_rsrc((void*)kb, 0, HEAD_BYTES, RSRC);
    const unsigned first = (s / QS) * (NT * 2 * PIECE) + ((s % QS) * NW + wave) * 4096u;
    unsigned cur = first;
#define LD(n, r_, so_) B[n] = __builtin_amdgcn_raw_buffer_load_b128(r_, voff + ((n) & 3) * 1024, (so_) + ((n) >> 2) * PIECE, AUX_NT)
#pragma unroll
    for (int n = 0; n < WIN; ++n) {
      if (n == PRO) asm volatile("s_barrier" ::: "memory");
      if (n >= PRO) asm volatile("s_waitcnt vmcnt(%0)" :: "n"(PRO - 1) : "memory");
      LD(n, rk, cur); FENCE();
    }
    int oaddr = (L_O + wave * 2 * D + (r >> 1) * D + 8 * c + 4 * (r & 1)) / 4, paddr = (tid >> 4) * 256 + s * 16 + c;
    asm volatile("" : "+v"(oaddr), "+v"(paddr));
    int boost = 0;
    unsigned due = TS * 64u;
#define KN(n) ((((n) & 1) << 2) | ((n) & 2) | (((n) >> 2) & 1))
#define REFILL(n) do { \
      asm volatile("s_cmp_gt_i32 %0, %1\n\ts_cbranch_scc1 1f\n\ts_waitcnt vmcnt(%4)\n\t" \
                   "s_cmp_gt_i32 %0, %2\n\ts_cbranch_scc1 1f\n\ts_waitcnt vmcnt(%5)\n\t" \
                   "s_cmp_gt_i32 %0, %3\n\ts_cbranch_scc1 1f\n\ts_waitcnt vmcnt(%6)\n1:" \
                   :: "s"(boost), "n"(16 + KN(n)), "n"(8 + KN(n)), "n"(KN(n)), "n"(WT + 1), "n"(WT), "n"(WT - 1) : "scc", "memory"); \
      if ((n) + WIN < 8) { LD(((n) + WIN) & 7, rc, cur); } else { LD(((n) + WIN) & 7, rn, nxt); } FENCE(); } while (0)
#define PACE(sprint) do { asm volatile("" : "+s"(now), "+v"(ref)); \
      const int late = (int)(((unsigned)now - (unsigned)__builtin_amdgcn_readfirstlane(ref)) * 64u - due); \
      due += (TE - TS) * 64u / (2 * NT); \
      boost = (sprint) ? 24 : late >> 8; FENCE(); } while (0)

    float m = -INFINITY;
    float* sw = lds + L_S + wave * (NT * 64) + r * 16 + (c & 8) + 2 * (c & 2) + ((c >> 1) & 2) + (c & 1);
#pragma unroll 1
    for (int it = 1; it <= NT; ++it) {
      u64 now = __builtin_amdgcn_s_memrealtime();
      unsigned ref = lds_u[L_REF];
      const bool more = it < NT;
      const unsigned nxt = more ? cur + 2 * PIECE : first;
      const auto rc = rk;
      const auto rn = __builtin_amdgcn_make_buffer_rsrc((void*)(more ? kb : vb), 0, HEAD_BYTES, RSRC);
      float y[8];
#define QKSTEP(i) { REFILL(i); const u32x4 kk = B[i]; float a = 0.f, b = 0.f; \
      _Pragma("unroll") for (int k = 0; k < 4; ++k) { a = dot2(kk[k], qa[k], a); b = dot2(kk[k], qb[k], b); } \
      y[i] = a + dpp<XOR1>(b); asm volatile("" : "+v"(y[i])); FENCE(); }
      QKSTEP(0) QKSTEP(1) QKSTEP(2) QKSTEP(3)
      PACE(false);
      QKSTEP(4) QKSTEP(5) QKSTEP(6) QKSTEP(7)
      float z[4], u0, u1, sc;
#pragma unroll
      for (int i = 0; i < 4; ++i) { const float t1 = y[i] + dpp<XOR8>(y[i]), t2 = y[i + 4] + dpp<XOR8>(y[i + 4]); z[i] = (c & 8) ? t2 : t1; }
      { const float t1 = z[0] + dpp<XOR2>(z[0]), t2 = z[2] + dpp<XOR2>(z[2]); u0 = (c & 2) ? t2 : t1; }
      { const float t1 = z[1] + dpp<XOR2>(z[1]), t2 = z[3] + dpp<XOR2>(z[3]); u1 = (c & 2) ? t2 : t1; }
      { const float t1 = u0 + dpp<SHL4>(u0), t2 = u1 + dpp<SHR4>(u1); sc = (c & 4) ? t2 : t1; }
      sc *= SCALE * LOG2E;
      m = fmaxf(m, sc);
      *sw = sc; sw += 64;
      cur = nxt;
    }
    m = fmaxf(m, dpp<XOR2>(m)); m = fmaxf(m, dpp<ROR4>(m)); m = fmaxf(m, dpp<XOR8>(m)); m = xrow_max(m);
    const f32x2 mneg = {-dpp<BCAST + 0>(m), -dpp<BCAST + 1>(m)};

    f32x2 acc[8], lsum = {0.f, 0.f};
#pragma unroll
    for (int k = 0; k < 8; ++k) acc[k] = f32x2{0.f, 0.f};
    const f32x4* sr = reinterpret_cast<const f32x4*>(lds + L_S + wave * (NT * 64) + r * 16);
    const auto rv = __builtin_amdgcn_make_buffer_rsrc((void*)vb, 0, HEAD_BYTES, RSRC);
#pragma unroll 1
    for (int it = 1; it <= NT; ++it) {
      u64 now = __builtin_amdgcn_s_memrealtime();
      unsigned ref = lds_u[L_REF];
      const f32x4 s4[4] = {sr[0], sr[1], sr[2], sr[3]};
      sr += 16;
      const bool more = it < NT;
      const unsigned nxt = more ? cur + 2 * PIECE : 0u;
      const auto rc = rv;
      const auto rn = __builtin_amdgcn_make_buffer_rsrc((void*)vb, 0, more ? HEAD_BYTES : 0u, RSRC);
#define PVSTEP(i) { REFILL(i); const f32x2 e = f32x2{s4[(i) >> 1][2 * ((i) & 1)], s4[(i) >> 1][2 * ((i) & 1) + 1]} + mneg; \
      const f32x2 pp = {__builtin_amdgcn_exp2f(e[0]), __builtin_amdgcn_exp2f(e[1])}; lsum += pp; \
      const u32x4 vv = B[i]; \
      _Pragma("unroll") for (int k = 0; k < 4; ++k) { const float lo = bflo(vv[k]), hi = bfhi(vv[k]); \
        acc[2 * k] = __builtin_elementwise_fma(pp, f32x2{lo, lo}, acc[2 * k]); \
        acc[2 * k + 1] = __builtin_elementwise_fma(pp, f32x2{hi, hi}, acc[2 * k + 1]); } \
      asm volatile("" : "+v"(acc[0]), "+v"(acc[1]), "+v"(acc[2]), "+v"(acc[3]), "+v"(acc[4]), "+v"(acc[5]), "+v"(acc[6]), "+v"(acc[7])); \
      FENCE(); }
      PVSTEP(0) PVSTEP(1) PVSTEP(2) PVSTEP(3)
      PACE(it + SPRINT > NT);
      PVSTEP(4) PVSTEP(5) PVSTEP(6) PVSTEP(7)
      cur = nxt;
    }

    float a0[8], a1[8];
#pragma unroll
    for (int k = 0; k < 8; ++k) { a0[k] = acc[k][0]; a1[k] = acc[k][1]; }
    float l = rs32(lsum[0], lsum[1]); l = rs16(l, l);
    asm volatile("s_nop 1\n\tv_permlane32_swap_b32 %0, %8\n\tv_permlane32_swap_b32 %1, %9\n\tv_permlane32_swap_b32 %2, %10\n\tv_permlane32_swap_b32 %3, %11\n\t"
                 "v_permlane32_swap_b32 %4, %12\n\tv_permlane32_swap_b32 %5, %13\n\tv_permlane32_swap_b32 %6, %14\n\tv_permlane32_swap_b32 %7, %15\n\t"
                 "v_add_f32 %0, %0, %8\n\tv_add_f32 %4, %4, %12\n\tv_add_f32 %1, %1, %9\n\tv_add_f32 %5, %5, %13\n\t"
                 "v_add_f32 %2, %2, %10\n\tv_add_f32 %6, %6, %14\n\tv_add_f32 %3, %3, %11\n\tv_add_f32 %7, %7, %15\n\t"
                 "v_permlane16_swap_b32 %0, %4\n\tv_permlane16_swap_b32 %1, %5\n\tv_permlane16_swap_b32 %2, %6\n\tv_permlane16_swap_b32 %3, %7"
                 : "+v"(a0[0]), "+v"(a0[1]), "+v"(a0[2]), "+v"(a0[3]), "+v"(a0[4]), "+v"(a0[5]), "+v"(a0[6]), "+v"(a0[7]),
                   "+v"(a1[0]), "+v"(a1[1]), "+v"(a1[2]), "+v"(a1[3]), "+v"(a1[4]), "+v"(a1[5]), "+v"(a1[6]), "+v"(a1[7]));
    reinterpret_cast<f32x4*>(lds)[oaddr] = f32x4{a0[0] + a0[4], a0[1] + a0[5], a0[2] + a0[6], a0[3] + a0[7]};
    if ((lane & 31) == (lane >> 5)) { lds[L_ML + (lane >> 5) * 2 * NW + wave] = m; lds[L_ML + (lane >> 5) * 2 * NW + NW + wave] = l; }
    __syncthreads();
    const f32x4 mm = *reinterpret_cast<const f32x4*>(lds + L_ML + (tid >> 7) * 2 * NW);
    const f32x4 ll = *reinterpret_cast<const f32x4*>(lds + L_ML + (tid >> 7) * 2 * NW + NW);
    float oo[NW];
#pragma unroll
    for (int w = 0; w < NW; ++w) oo[w] = lds[L_O + w * 2 * D + tid];
    const unsigned tag = lds_u[L_TAG + (tid >> 4)];
    FENCE();
    const float M = fmaxf(fmaxf(mm[0], mm[1]), fmaxf(mm[2], mm[3]));
    float L = 0.f, o = 0.f;
#pragma unroll
    for (int w = 0; w < NW; ++w) { const float wt = __builtin_amdgcn_exp2f(mm[w] - M); L = fmaf(wt, ll[w], L); o = fmaf(wt, oo[w], o); }
    const unsigned ov = (__float_as_uint(o * __builtin_amdgcn_rcpf(L)) & ~31u) | tag;
    __hip_atomic_store(inbox + (unsigned)paddr, pack(ov, __float_as_uint(M + __builtin_amdgcn_logf(L))), RLX_AGENT);
  }
}

extern "C" void kernel_launch(void* const* d_in, const int* in_sizes, int n_in,
                              void* d_out, int out_size, void* d_ws, size_t ws_size,
                              hipStream_t stream) {
  attn_fused<<<HKV * NSPLIT, NTHR, 0, stream>>>((const unsigned short*)d_in[0], (const unsigned short*)d_in[1], (const unsigned short*)d_in[2],
                                               (const unsigned short*)d_in[3], (const unsigned short*)d_in[4], (const unsigned short*)d_in[5],
                                               (unsigned short*)d_out, (u64*)d_ws);
}
```

```cpp
#include <hip/hip_runtime.h>
#include <hip/hip_bf16.h>
#include <stdint.h>

typedef unsigned u32x2 __attribute__((ext_vector_type(2)));
typedef unsigned u32x4 __attribute__((ext_vector_type(4)));
typedef float f32x2 __attribute__((ext_vector_type(2)));
typedef float f32x4 __attribute__((ext_vector_type(4)));
typedef __bf16 bf16x2 __attribute__((ext_vector_type(2)));
typedef unsigned long long u64;
typedef __attribute__((address_space(1))) u64 gu64;
#define LAS __attribute__((address_space(3)))
#define RLX_AGENT __ATOMIC_RELAXED, __HIP_MEMORY_SCOPE_AGENT
#define FENCE() __builtin_amdgcn_sched_barrier(0)

constexpr int HKV = 16, CTX = 32768, D = 128;
constexpr int NSPLIT = 16;
constexpr int NW = 4;
constexpr int NTHR = (NW + 1) * 64;
constexpr int NT = CTX / NSPLIT / NW / 32;
constexpr int QS = 4;
constexpr unsigned HEAD_BYTES = CTX * D * 2;
constexpr unsigned PIECE = QS * NW * 4096u;
constexpr unsigned RSRC = 0x00020000;
constexpr int AUX_NT = 2, AUX_SC1 = 16;
constexpr float SCALE = 0.088388347648318440f;
constexpr float LOG2E = 1.4426950408889634f;
constexpr int WIN = 7;
constexpr int WT = 5, PRO = 4;
constexpr unsigned TS = 205, TE = 3530;
constexpr int SPRINT = 2;
constexpr unsigned SPIN_CAP = 1u << 16;
constexpr int WS_IN = 0, WS_X = HKV * NSPLIT * NSPLIT * 16;
constexpr int L_O = 0, L_ML = NW * 2 * D, L_TAG = L_ML + 4 * NW, L_REF = L_TAG + NSPLIT, L_S = L_REF + 4, L_END = L_S + NW * NT * 64;

__device__ __forceinline__ float dot2(unsigned k, unsigned q, float acc) {
  return __builtin_amdgcn_fdot2_f32_bf16(__builtin_bit_cast(bf16x2, k), __builtin_bit_cast(bf16x2, q), acc, false);
}
template <int CTRL> __device__ __forceinline__ int idpp(int x) { return __builtin_amdgcn_mov_dpp(x, CTRL, 0xf, 0xf, true); }
template <int CTRL> __device__ __forceinline__ float dpp(float x) { return __builtin_bit_cast(float, idpp<CTRL>(__builtin_bit_cast(int, x))); }
constexpr int XOR1 = 0xB1, XOR2 = 0x4E, XOR7 = 0x141, XOR8 = 0x128, ROR4 = 0x124, SHL4 = 0x104, SHR4 = 0x114, BCAST = 0x150;
__device__ __forceinline__ float bflo(unsigned x) { return __builtin_bit_cast(float, x << 16); }
__device__ __forceinline__ float bfhi(unsigned x) { return __builtin_bit_cast(float, x & 0xffff0000u); }
__device__ __forceinline__ float rs32(float a, float b) {
  auto t = __builtin_amdgcn_permlane32_swap(__float_as_uint(a), __float_as_uint(b), false, false);
  return __uint_as_float(t[0]) + __uint_as_float(t[1]);
}
__device__ __forceinline__ float rs16(float a, float b) {
  auto t = __builtin_amdgcn_permlane16_swap(__float_as_uint(a), __float_as_uint(b), false, false);
  return __uint_as_float(t[0]) + __uint_as_float(t[1]);
}
__device__ __forceinline__ float xrow_sum(float x) { x = rs16(x, x); return rs32(x, x); }
__device__ __forceinline__ float xrow_max(float x) {
  auto s = __builtin_amdgcn_permlane16_swap(__float_as_uint(x), __float_as_uint(x), false, false);
  x = fmaxf(__uint_as_float(s[0]), __uint_as_float(s[1]));
  auto t = __builtin_amdgcn_permlane32_swap(__float_as_uint(x), __float_as_uint(x), false, false);
  return fmaxf(__uint_as_float(t[0]), __uint_as_float(t[1]));
}
__device__ __forceinline__ u64 pack(unsigned lo, unsigned hi) { return ((u64)hi << 32) | lo; }

__global__ void __launch_bounds__(NTHR, 2)
attn_fused(const unsigned short* __restrict__ q, const unsigned short* __restrict__ kc, const unsigned short* __restrict__ vc,
           const unsigned short* __restrict__ kt, const unsigned short* __restrict__ vt, const unsigned short* __restrict__ mask,
           unsigned short* __restrict__ out, u64* ws) {
  __shared__ __attribute__((aligned(16))) float lds[L_END];
  volatile LAS unsigned* const lds_u = (volatile LAS unsigned*)lds;
  asm volatile("" :: "s"(q), "s"(kc), "s"(vc), "s"(kt), "s"(vt), "s"(mask), "s"(out), "s"(ws));
  const int wg = blockIdx.x, s = wg >> 4, h = (wg - s) & 15;
  const int tid = threadIdx.x, lane = tid & 63, wave = __builtin_amdgcn_readfirstlane(tid >> 6), r = lane >> 4, c = lane & 15;
  gu64* const inbox = (gu64*)ws + WS_IN + h * (NSPLIT * NSPLIT * 16);

  if (wave == NW) {
    const unsigned t_own = (unsigned)__builtin_amdgcn_s_memrealtime();
    gu64* const xin = (gu64*)ws + WS_X + (h * NSPLIT + c) * NSPLIT + s;
    gu64* const xout = (gu64*)ws + WS_X + (h * NSPLIT + s) * NSPLIT + c;
    gu64* const mybox = inbox + s * 256 + lane;
    u64 x = __hip_atomic_load(xin, RLX_AGENT);
    u32x2 found[4];
#pragma unroll
    for (int i = 0; i < 4; ++i)
      found[i] = __builtin_amdgcn_raw_buffer_load_b64(__builtin_amdgcn_make_buffer_rsrc((void*)(inbox + s * 256), 0, 2048, RSRC),
                                                      lane * 8 + i * 512, 0, AUX_SC1 | AUX_NT);
    lds_u[L_REF] = t_own;
    asm volatile("s_waitcnt lgkmcnt(0)\n\ts_barrier" ::: "memory");
    const unsigned qq = *reinterpret_cast<const unsigned*>(q + (2 * h + (s >> 3)) * D + 2 * lane);
    const unsigned kk = *reinterpret_cast<const unsigned*>(kt + h * D + 2 * lane);
    float tree_v = bflo(vt[h * D + (s & 7) * 16 + c]);
    const float mk = bflo(mask[0]);
    unsigned want[4], mytag = 0;
#pragma unroll
    for (int i = 0; i < 4; ++i) {
      int used = 1 << (found[i][0] & 31u);
      used |= idpp<XOR1>(used); used |= idpp<XOR2>(used); used |= idpp<XOR7>(used); used |= idpp<XOR8>(used);
      want[i] = __builtin_ctz(~used);
      const unsigned t = __shfl(want[i], (c & 3) * 16);
      mytag = (c >> 2) == i ? t : mytag;
    }
    unsigned mine = (t_own << 5) | mytag;
    mine += (mine == (unsigned)(x >> 32)) ? 32u : 0u;
    unsigned echoed = (unsigned)x;
    if (lane < 16) __hip_atomic_store(xout, pack(mine, echoed), RLX_AGENT);
    float d = dot2(qq, kk, 0.f);
    d += dpp<XOR1>(d); d += dpp<XOR2>(d); d += dpp<XOR7>(d); d += dpp<XOR8>(d);
    d = xrow_sum(d);
    float tree_s = (d * SCALE + (1.0f - mk) * -1000000000.0f) * LOG2E;
    bool ok = false;
    for (unsigned spin = 0; spin < SPIN_CAP && !ok; ++spin) {
      __builtin_amdgcn_s_sleep(127);
      x = __hip_atomic_load(xin, RLX_AGENT);
      if (__any((unsigned)x != echoed)) { echoed = (unsigned)x; if (lane < 16) __hip_atomic_store(xout, pack(mine, echoed), RLX_AGENT); }
      ok = __all((unsigned)(x >> 32) == mine);
    }
    {
      int dt = min(max((int)(echoed - (t_own << 5)) >> 5, -1000), 1000);
      dt += idpp<XOR1>(dt); dt += idpp<XOR2>(dt); dt += idpp<XOR7>(dt); dt += idpp<XOR8>(dt);
      lds_u[L_REF] = t_own + (dt >> 4);
      lds_u[L_TAG + c] = echoed & 31u;
    }
    unsigned oi = (2 * h + (s >> 3)) * D + (s & 7) * 16 + c;
    asm volatile("" : "+v"(tree_s), "+v"(tree_v), "+v"(oi));
    __syncthreads();
    __builtin_amdgcn_s_sleep(20);
    u64 g[4] = {0, 0, 0, 0};
    bool got = false;
    for (unsigned spin = 0; spin < 16 * SPIN_CAP && ok && !got; ++spin) {
#pragma unroll
      for (int i = 0; i < 4; ++i) g[i] = __hip_atomic_load(mybox + i * 64, RLX_AGENT);
      got = __all(((((unsigned)g[0] ^ want[0]) | ((unsigned)g[1] ^ want[1]) | ((unsigned)g[2] ^ want[2]) | ((unsigned)g[3] ^ want[3])) & 31u) == 0u);
    }
    float ls[4], ov[4];
#pragma unroll
    for (int i = 0; i < 4; ++i) { ls[i] = __uint_as_float((unsigned)(g[i] >> 32)); ov[i] = __uint_as_float((unsigned)g[i] & ~31u); }
    const float M = fmaxf(xrow_max(fmaxf(fmaxf(ls[0], ls[1]), fmaxf(ls[2], ls[3]))), tree_s);
    const float pt = __builtin_amdgcn_exp2f(tree_s - M);
    float L = 0.f, o = 0.f;
#pragma unroll
    for (int i = 0; i < 4; ++i) { const float wt = __builtin_amdgcn_exp2f(ls[i] - M); L += wt; o = fmaf(wt, ov[i], o); }
    L = xrow_sum(L); o = xrow_sum(o);
    float res = fmaf(pt, tree_v, o) * __builtin_amdgcn_rcpf(L + pt);
    if (!got) res = __uint_as_float(0x7fc00000u);
    if (lane < 16) out[oi] = __builtin_bit_cast(unsigned short, (__bf16)res);
  } else {
    const unsigned short* kb = kc + (size_t)h * CTX * D;
    const unsigned short* vb = vc + (size_t)h * CTX * D;
    const unsigned voff = lane * 16;
    u32x4 B[8];
    const auto rk = __builtin_amdgcn_make_buffer_rsrc((void*)kb, 0, HEAD_BYTES, RSRC);
    const unsigned first = (s / QS) * (NT * 2 * PIECE) + ((s % QS) * NW + wave) * 4096u;
    unsigned cur = first;
#define LD(n, r_, so_) B[n] = __builtin_amdgcn_raw_buffer_load_b128(r_, voff + ((n) & 3) * 1024, (so_) + ((n) >> 2) * PIECE, AUX_NT)
#pragma unroll
    for (int n = 0; n < PRO; ++n) { LD(n, rk, cur); FENCE(); }
    u32x4 qa = *reinterpret_cast<const u32x4*>(q + (2 * h + (c & 1)) * D + 8 * c);
    u32x4 qb = *reinterpret_cast<const u32x4*>(q + (2 * h + 1 - (c & 1)) * D + 8 * c);
    FENCE();
    asm volatile("s_barrier" ::: "memory");
#pragma unroll
    for (int n = PRO; n < WIN; ++n) { asm volatile("s_waitcnt vmcnt(%0)" :: "n"(PRO + 1) : "memory"); LD(n, rk, cur); FENCE(); }
    asm volatile("" : "+v"(qa), "+v"(qb));
    int oaddr = (L_O + wave * 2 * D + (r >> 1) * D + 8 * c + 4 * (r & 1)) / 4, paddr = (tid >> 4) * 256 + s * 16 + c;
    asm volatile("" : "+v"(oaddr), "+v"(paddr));
    int boost = 0;
    unsigned due = TS * 64u;
#define KN(n) ((((n) & 1) << 2) | ((n) & 2) | (((n) >> 2) & 1))
#define REFILL(n) do { \
      asm volatile("s_cmp_gt_i32 %0, %1\n\ts_cbranch_scc1 1f\n\ts_waitcnt vmcnt(%4)\n\t" \
                   "s_cmp_gt_i32 %0, %2\n\ts_cbranch_scc1 1f\n\ts_waitcnt vmcnt(%5)\n\t" \
                   "s_cmp_gt_i32 %0, %3\n\ts_cbranch_scc1 1f\n\ts_waitcnt vmcnt(%6)\n1:" \
                   :: "s"(boost), "n"(16 + KN(n)), "n"(8 + KN(n)), "n"(KN(n)), "n"(WT + 1), "n"(WT), "n"(WT - 1) : "scc", "memory"); \
      if ((n) + WIN < 8) { LD(((n) + WIN) & 7, rc, cur); } else { LD(((n) + WIN) & 7, rn, nxt); } FENCE(); } while (0)
#define PACE(sprint) do { asm volatile("" : "+s"(now), "+v"(ref)); \
      const int late = (int)(((unsigned)now - (unsigned)__builtin_amdgcn_readfirstlane(ref)) * 64u - due); \
      due += (TE - TS) * 64u / (2 * NT); \
      boost = (sprint) ? 14 + (late >> 7) : late >> 8; FENCE(); } while (0)

    float m = -INFINITY;
    float* sw = lds + L_S + wave * (NT * 64) + r * 16 + (c & 8) + 2 * (c & 2) + ((c >> 1) & 2) + (c & 1);
#pragma unroll 1
    for (int it = 1; it <= NT; ++it) {
      u64 now = __builtin_amdgcn_s_memrealtime();
      unsigned ref = lds_u[L_REF];
      const bool more = it < NT;
      const unsigned nxt = more ? cur + 2 * PIECE : first;
      const auto rc = rk;
      const auto rn = __builtin_amdgcn_make_buffer_rsrc((void*)(more ? kb : vb), 0, HEAD_BYTES, RSRC);
      float y[8];
#define QKSTEP(i) { REFILL(i); const u32x4 kk = B[i]; float a = 0.f, b = 0.f; \
      _Pragma("unroll") for (int k = 0; k < 4; ++k) { a = dot2(kk[k], qa[k], a); b = dot2(kk[k], qb[k], b); } \
      y[i] = a + dpp<XOR1>(b); asm volatile("" : "+v"(y[i])); FENCE(); }
      QKSTEP(0) QKSTEP(1) QKSTEP(2) QKSTEP(3)
      PACE(false);
      QKSTEP(4) QKSTEP(5) QKSTEP(6) QKSTEP(7)
      float z[4], u0, u1, sc;
#pragma unroll
      for (int i = 0; i < 4; ++i) { const float t1 = y[i] + dpp<XOR8>(y[i]), t2 = y[i + 4] + dpp<XOR8>(y[i + 4]); z[i] = (c & 8) ? t2 : t1; }
      { const float t1 = z[0] + dpp<XOR2>(z[0]), t2 = z[2] + dpp<XOR2>(z[2]); u0 = (c & 2) ? t2 : t1; }
      { const float t1 = z[1] + dpp<XOR2>(z[1]), t2 = z[3] + dpp<XOR2>(z[3]); u1 = (c & 2) ? t2 : t1; }
      { const float t1 = u0 + dpp<SHL4>(u0), t2 = u1 + dpp<SHR4>(u1); sc = (c & 4) ? t2 : t1; }
      sc *= SCALE * LOG2E;
      m = fmaxf(m, sc);
      *sw = sc; sw += 64;
      cur = nxt;
    }
    m = fmaxf(m, dpp<XOR2>(m)); m = fmaxf(m, dpp<ROR4>(m)); m = fmaxf(m, dpp<XOR8>(m)); m = xrow_max(m);
    const f32x2 mneg = {-dpp<BCAST + 0>(m), -dpp<BCAST + 1>(m)};

    f32x2 acc[8], lsum = {0.f, 0.f};
#pragma unroll
    for (int k = 0; k < 8; ++k) acc[k] = f32x2{0.f, 0.f};
    const f32x4* sr = reinterpret_cast<const f32x4*>(lds + L_S + wave * (NT * 64) + r * 16);
    const auto rv = __builtin_amdgcn_make_buffer_rsrc((void*)vb, 0, HEAD_BYTES, RSRC);
#pragma unroll 1
    for (int it = 1; it <= NT; ++it) {
      u64 now = __builtin_amdgcn_s_memrealtime();
      unsigned ref = lds_u[L_REF];
      const f32x4 s4[4] = {sr[0], sr[1], sr[2], sr[3]};
      sr += 16;
      const bool more = it < NT;
      const unsigned nxt = more ? cur + 2 * PIECE : 0u;
      const auto rc = rv;
      const auto rn = __builtin_amdgcn_make_buffer_rsrc((void*)vb, 0, more ? HEAD_BYTES : 0u, RSRC);
#define PVSTEP(i) { REFILL(i); const f32x2 e = f32x2{s4[(i) >> 1][2 * ((i) & 1)], s4[(i) >> 1][2 * ((i) & 1) + 1]} + mneg; \
      const f32x2 pp = {__builtin_amdgcn_exp2f(e[0]), __builtin_amdgcn_exp2f(e[1])}; lsum += pp; \
      const u32x4 vv = B[i]; \
      _Pragma("unroll") for (int k = 0; k < 4; ++k) { const float lo = bflo(vv[k]), hi = bfhi(vv[k]); \
        acc[2 * k] = __builtin_elementwise_fma(pp, f32x2{lo, lo}, acc[2 * k]); \
        acc[2 * k + 1] = __builtin_elementwise_fma(pp, f32x2{hi, hi}, acc[2 * k + 1]); } \
      asm volatile("" : "+v"(acc[0]), "+v"(acc[1]), "+v"(acc[2]), "+v"(acc[3]), "+v"(acc[4]), "+v"(acc[5]), "+v"(acc[6]), "+v"(acc[7])); \
      FENCE(); }
      PVSTEP(0) PVSTEP(1) PVSTEP(2) PVSTEP(3)
      PACE(it + SPRINT > NT);
      PVSTEP(4) PVSTEP(5) PVSTEP(6) PVSTEP(7)
      cur = nxt;
    }

    float a0[8], a1[8];
#pragma unroll
    for (int k = 0; k < 8; ++k) { a0[k] = acc[k][0]; a1[k] = acc[k][1]; }
    float l = rs32(lsum[0], lsum[1]); l = rs16(l, l);
    asm volatile("s_nop 1\n\tv_permlane32_swap_b32 %0, %8\n\tv_permlane32_swap_b32 %1, %9\n\tv_permlane32_swap_b32 %2, %10\n\tv_permlane32_swap_b32 %3, %11\n\t"
                 "v_permlane32_swap_b32 %4, %12\n\tv_permlane32_swap_b32 %5, %13\n\tv_permlane32_swap_b32 %6, %14\n\tv_permlane32_swap_b32 %7, %15\n\t"
                 "v_add_f32 %0, %0, %8\n\tv_add_f32 %4, %4, %12\n\tv_add_f32 %1, %1, %9\n\tv_add_f32 %5, %5, %13\n\t"
                 "v_add_f32 %2, %2, %10\n\tv_add_f32 %6, %6, %14\n\tv_add_f32 %3, %3, %11\n\tv_add_f32 %7, %7, %15\n\t"
                 "v_permlane16_swap_b32 %0, %4\n\tv_permlane16_swap_b32 %1, %5\n\tv_permlane16_swap_b32 %2, %6\n\tv_permlane16_swap_b32 %3, %7"
                 : "+v"(a0[0]), "+v"(a0[1]), "+v"(a0[2]), "+v"(a0[3]), "+v"(a0[4]), "+v"(a0[5]), "+v"(a0[6]), "+v"(a0[7]),
                   "+v"(a1[0]), "+v"(a1[1]), "+v"(a1[2]), "+v"(a1[3]), "+v"(a1[4]), "+v"(a1[5]), "+v"(a1[6]), "+v"(a1[7]));
    reinterpret_cast<f32x4*>(lds)[oaddr] = f32x4{a0[0] + a0[4], a0[1] + a0[5], a0[2] + a0[6], a0[3] + a0[7]};
    if ((lane & 31) == (lane >> 5)) { lds[L_ML + (lane >> 5) * 2 * NW + wave] = m; lds[L_ML + (lane >> 5) * 2 * NW + NW + wave] = l; }
    __syncthreads();
    const f32x4 mm = *reinterpret_cast<const f32x4*>(lds + L_ML + (tid >> 7) * 2 * NW);
    const f32x4 ll = *reinterpret_cast<const f32x4*>(lds + L_ML + (tid >> 7) * 2 * NW + NW);
    float oo[NW];
#pragma unroll
    for (int w = 0; w < NW; ++w) oo[w] = lds[L_O + w * 2 * D + tid];
    const unsigned tag = lds_u[L_TAG + (tid >> 4)];
    FENCE();
    const float M = fmaxf(fmaxf(mm[0], mm[1]), fmaxf(mm[2], mm[3]));
    float L = 0.f, o = 0.f;
#pragma unroll
    for (int w = 0; w < NW; ++w) { const float wt = __builtin_amdgcn_exp2f(mm[w] - M); L = fmaf(wt, ll[w], L); o = fmaf(wt, oo[w], o); }
    const unsigned ov = (__float_as_uint(o * __builtin_amdgcn_rcpf(L)) & ~31u) | tag;
    __hip_atomic_store(inbox + (unsigned)paddr, pack(ov, __float_as_uint(M + __builtin_amdgcn_logf(L))), RLX_AGENT);
  }
}

extern "C" void kernel_launch(void* const* d_in, const int* in_sizes, int n_in,
                              void* d_out, int out_size, void* d_ws, size_t ws_size,
                              hipStream_t stream) {
  attn_fused<<<HKV * NSPLIT, NTHR, 0, stream>>>((const unsigned short*)d_in[0], (const unsigned short*)d_in[1], (const unsigned short*)d_in[2],
                                               (const unsigned short*)d_in[3], (const unsigned short*)d_in[4], (const unsigned short*)d_in[5],
                                               (unsigned short*)d_out, (u64*)d_ws);
}
```

```cpp
#include <hip/hip_runtime.h>
#include <hip/hip_bf16.h>
#include <stdint.h>

typedef unsigned u32x2 __attribute__((ext_vector_type(2)));
typedef unsigned u32x4 __attribute__((ext_vector_type(4)));
typedef float f32x2 __attribute__((ext_vector_type(2)));
typedef float f32x4 __attribute__((ext_vector_type(4)));
typedef __bf16 bf16x2 __attribute__((ext_vector_type(2)));
typedef unsigned long long u64;
typedef __attribute__((address_space(1))) u64 gu64;
#define LAS __attribute__((address_space(3)))
#define RLX_AGENT __ATOMIC_RELAXED, __HIP_MEMORY_SCOPE_AGENT
#define FENCE() __builtin_amdgcn_sched_barrier(0)

constexpr int HKV = 16, CTX = 32768, D = 128;
constexpr int NSPLIT = 16;
constexpr int NW = 4;
constexpr int NTHR = (NW + 1) * 64;
constexpr int NT = CTX / NSPLIT / NW / 32;
constexpr int QS = 4;
constexpr unsigned HEAD_BYTES = CTX * D * 2;
constexpr unsigned PIECE = QS * NW * 4096u;
constexpr unsigned RSRC = 0x00020000;
constexpr int AUX_NT = 2, AUX_SC1 = 16;
constexpr float SCALE = 0.088388347648318440f;
constexpr float LOG2E = 1.4426950408889634f;
constexpr int WIN = 7;
constexpr int WT = 5, PRO = 3;
constexpr unsigned TS = 135, TE = 3472;
constexpr unsigned TURN = 10;
constexpr int CALM = 5;
constexpr int SPRINT = 2;
constexpr unsigned SPIN_CAP = 1u << 16;
constexpr int WS_IN = 0, WS_X = HKV * NSPLIT * NSPLIT * 16;
constexpr int L_O = 0, L_ML = NW * 2 * D, L_TAG = L_ML + 4 * NW, L_REF = L_TAG + NSPLIT, L_S = L_REF + 4, L_END = L_S + NW * NT * 64;

__device__ __forceinline__ float dot2(unsigned k, unsigned q, float acc) {
  return __builtin_amdgcn_fdot2_f32_bf16(__builtin_bit_cast(bf16x2, k), __builtin_bit_cast(bf16x2, q), acc, false);
}
template <int CTRL> __device__ __forceinline__ int idpp(int x) { return __builtin_amdgcn_mov_dpp(x, CTRL, 0xf, 0xf, true); }
template <int CTRL> __device__ __forceinline__ float dpp(float x) { return __builtin_bit_cast(float, idpp<CTRL>(__builtin_bit_cast(int, x))); }
constexpr int XOR1 = 0xB1, XOR2 = 0x4E, XOR7 = 0x141, XOR8 = 0x128, ROR4 = 0x124, SHL4 = 0x104, SHR4 = 0x114, BCAST = 0x150;
__device__ __forceinline__ float bflo(unsigned x) { return __builtin_bit_cast(float, x << 16); }
__device__ __forceinline__ float bfhi(unsigned x) { return __builtin_bit_cast(float, x & 0xffff0000u); }
__device__ __forceinline__ float rs32(float a, float b) {
  auto t = __builtin_amdgcn_permlane32_swap(__float_as_uint(a), __float_as_uint(b), false, false);
  return __uint_as_float(t[0]) + __uint_as_float(t[1]);
}
__device__ __forceinline__ float rs16(float a, float b) {
  auto t = __builtin_amdgcn_permlane16_swap(__float_as_uint(a), __float_as_uint(b), false, false);
  return __uint_as_float(t[0]) + __uint_as_float(t[1]);
}
__device__ __forceinline__ float xrow_sum(float x) { x = rs16(x, x); return rs32(x, x); }
__device__ __forceinline__ float xrow_max(float x) {
  auto s = __builtin_amdgcn_permlane16_swap(__float_as_uint(x), __float_as_uint(x), false, false);
  x = fmaxf(__uint_as_float(s[0]), __uint_as_float(s[1]));
  auto t = __builtin_amdgcn_permlane32_swap(__float_as_uint(x), __float_as_uint(x), false, false);
  return fmaxf(__uint_as_float(t[0]), __uint_as_float(t[1]));
}
__device__ __forceinline__ u64 pack(unsigned lo, unsigned hi) { return ((u64)hi << 32) | lo; }

__global__ void __launch_bounds__(NTHR, 2)
attn_fused(const unsigned short* __restrict__ q, const unsigned short* __restrict__ kc, const unsigned short* __restrict__ vc,
           const unsigned short* __restrict__ kt, const unsigned short* __restrict__ vt, const unsigned short* __restrict__ mask,
           unsigned short* __restrict__ out, u64* ws) {
  __shared__ __attribute__((aligned(16))) float lds[L_END];
  volatile LAS unsigned* const lds_u = (volatile LAS unsigned*)lds;
  asm volatile("" :: "s"(q), "s"(kc), "s"(vc), "s"(kt), "s"(vt), "s"(mask), "s"(out), "s"(ws));
  const int wg = blockIdx.x, s = wg >> 4, h = (wg - s) & 15;
  const int tid = threadIdx.x, lane = tid & 63, wave = __builtin_amdgcn_readfirstlane(tid >> 6), r = lane >> 4, c = lane & 15;
  gu64* const inbox = (gu64*)ws + WS_IN + h * (NSPLIT * NSPLIT * 16);

  if (wave == NW) {
    gu64* const xin = (gu64*)ws + WS_X + (h * NSPLIT + c) * NSPLIT + s;
    gu64* const xout = (gu64*)ws + WS_X + (h * NSPLIT + s) * NSPLIT + c;
    gu64* const mybox = inbox + s * 256 + lane;
    u64 x = __hip_atomic_load(xin, RLX_AGENT);
    u32x2 found[4];
#pragma unroll
    for (int i = 0; i < 4; ++i)
      found[i] = __builtin_amdgcn_raw_buffer_load_b64(__builtin_amdgcn_make_buffer_rsrc((void*)(inbox + s * 256), 0, 2048, RSRC),
                                                      lane * 8 + i * 512, 0, AUX_SC1 | AUX_NT);
    lds_u[L_REF + 1] = 0u;
    asm volatile("s_waitcnt lgkmcnt(0)\n\ts_barrier" ::: "memory");
    const unsigned qq = *reinterpret_cast<const unsigned*>(q + (2 * h + (s >> 3)) * D + 2 * lane);
    const unsigned kk = *reinterpret_cast<const unsigned*>(kt + h * D + 2 * lane);
    float tree_v = bflo(vt[h * D + (s & 7) * 16 + c]);
    const float mk = bflo(mask[0]);
    unsigned want[4], mytag = 0;
#pragma unroll
    for (int i = 0; i < 4; ++i) {
      int used = 1 << (found[i][0] & 31u);
      used |= idpp<XOR1>(used); used |= idpp<XOR2>(used); used |= idpp<XOR7>(used); used |= idpp<XOR8>(used);
      want[i] = __builtin_ctz(~used);
      const unsigned t = __shfl(want[i], (c & 3) * 16);
      mytag = (c >> 2) == i ? t : mytag;
    }
#pragma unroll 1
    for (unsigned i = 0; i < SPIN_CAP && __builtin_amdgcn_readfirstlane(lds_u[L_REF + 1]) == 0; ++i) __builtin_amdgcn_s_sleep(1);
    const unsigned t_own = __builtin_amdgcn_readfirstlane(lds_u[L_REF]);
    unsigned mine = (t_own << 5) | mytag;
    mine += (mine == (unsigned)(x >> 32)) ? 32u : 0u;
    unsigned echoed = (unsigned)x;
    if (lane < 16) __hip_atomic_store(xout, pack(mine, echoed), RLX_AGENT);
    float d = dot2(qq, kk, 0.f);
    d += dpp<XOR1>(d); d += dpp<XOR2>(d); d += dpp<XOR7>(d); d += dpp<XOR8>(d);
    d = xrow_sum(d);
    float tree_s = (d * SCALE + (1.0f - mk) * -1000000000.0f) * LOG2E;
    bool ok = false;
    for (unsigned spin = 0; spin < SPIN_CAP && !ok; ++spin) {
      __builtin_amdgcn_s_sleep(127);
      x = __hip_atomic_load(xin, RLX_AGENT);
      if (__any((unsigned)x != echoed)) { echoed = (unsigned)x; if (lane < 16) __hip_atomic_store(xout, pack(mine, echoed), RLX_AGENT); }
      ok = __all((unsigned)(x >> 32) == mine);
    }
    {
      int dt = min(max((int)(echoed - (t_own << 5)) >> 5, -1000), 1000);
      dt += idpp<XOR1>(dt); dt += idpp<XOR2>(dt); dt += idpp<XOR7>(dt); dt += idpp<XOR8>(dt);
      lds_u[L_REF] = t_own + (dt >> 4);
      lds_u[L_TAG + c] = echoed & 31u;
    }
    unsigned oi = (2 * h + (s >> 3)) * D + (s & 7) * 16 + c;
    asm volatile("" : "+v"(tree_s), "+v"(tree_v), "+v"(oi));
    __syncthreads();
    __builtin_amdgcn_s_sleep(20);
    u64 g[4] = {0, 0, 0, 0};
    bool got = false;
    for (unsigned spin = 0; spin < 16 * SPIN_CAP && ok && !got; ++spin) {
#pragma unroll
      for (int i = 0; i < 4; ++i) g[i] = __hip_atomic_load(mybox + i * 64, RLX_AGENT);
      got = __all(((((unsigned)g[0] ^ want[0]) | ((unsigned)g[1] ^ want[1]) | ((unsigned)g[2] ^ want[2]) | ((unsigned)g[3] ^ want[3])) & 31u) == 0u);
    }
    float ls[4], ov[4];
#pragma unroll
    for (int i = 0; i < 4; ++i) { ls[i] = __uint_as_float((unsigned)(g[i] >> 32)); ov[i] = __uint_as_float((unsigned)g[i] & ~31u); }
    const float M = fmaxf(xrow_max(fmaxf(fmaxf(ls[0], ls[1]), fmaxf(ls[2], ls[3]))), tree_s);
    const float pt = __builtin_amdgcn_exp2f(tree_s - M);
    float L = 0.f, o = 0.f;
#pragma unroll
    for (int i = 0; i < 4; ++i) { const float wt = __builtin_amdgcn_exp2f(ls[i] - M); L += wt; o = fmaf(wt, ov[i], o); }
    L = xrow_sum(L); o = xrow_sum(o);
    float res = fmaf(pt, tree_v, o) * __builtin_amdgcn_rcpf(L + pt);
    if (!got) res = __uint_as_float(0x7fc00000u);
    if (lane < 16) out[oi] = __builtin_bit_cast(unsigned short, (__bf16)res);
  } else {
    const unsigned short* kb = kc + (size_t)h * CTX * D;
    const unsigned short* vb = vc + (size_t)h * CTX * D;
    const unsigned voff = lane * 16;
    u32x4 B[8];
    const auto rk = __builtin_amdgcn_make_buffer_rsrc((void*)kb, 0, HEAD_BYTES, RSRC);
    const unsigned first = (s / QS) * (NT * 2 * PIECE) + ((s % QS) * NW + wave) * 4096u;
    unsigned cur = first;
#define LD(n, r_, so_) B[n] = __builtin_amdgcn_raw_buffer_load_b128(r_, voff + ((n) & 3) * 1024, (so_) + ((n) >> 2) * PIECE, AUX_NT)
#pragma unroll
    for (int n = 0; n < PRO; ++n) { LD(n, rk, cur); FENCE(); }
    u32x4 qa = *reinterpret_cast<const u32x4*>(q + (2 * h + (c & 1)) * D + 8 * c);
    u32x4 qb = *reinterpret_cast<const u32x4*>(q + (2 * h + 1 - (c & 1)) * D + 8 * c);
    FENCE();
    asm volatile("s_barrier" ::: "memory");
#pragma unroll
    for (int n = PRO; n < WIN; ++n) {
      asm volatile("s_waitcnt vmcnt(%0)" :: "n"(PRO + 1) : "memory"); LD(n, rk, cur); FENCE();
      if (n == PRO && wave == 0) { lds_u[L_REF] = (unsigned)__builtin_amdgcn_s_memrealtime(); lds_u[L_REF + 1] = 1u; }
      FENCE();
    }
    asm volatile("" : "+v"(qa), "+v"(qb));
    int oaddr = (L_O + wave * 2 * D + (r >> 1) * D + 8 * c + 4 * (r & 1)) / 4, paddr = (tid >> 4) * 256 + s * 16 + c;
    asm volatile("" : "+v"(oaddr), "+v"(paddr));
    int boost = 0;
    unsigned due = TS * 64u;
#define KN(n) ((((n) & 1) << 2) | ((n) & 2) | (((n) >> 2) & 1))
#define REFILL(n) do { \
      asm volatile("s_cmp_gt_i32 %0, %1\n\ts_cbranch_scc1 1f\n\ts_waitcnt vmcnt(%4)\n\t" \
                   "s_cmp_gt_i32 %0, %2\n\ts_cbranch_scc1 1f\n\ts_waitcnt vmcnt(%5)\n\t" \
                   "s_cmp_gt_i32 %0, %3\n\ts_cbranch_scc1 1f\n\ts_waitcnt vmcnt(%6)\n1:" \
                   :: "s"(boost), "n"(16 + KN(n)), "n"(8 + KN(n)), "n"(KN(n)), "n"(WT + 1), "n"(WT), "n"(WT - 1) : "scc", "memory"); \
      if ((n) + WIN < 8) { LD(((n) + WIN) & 7, rc, cur); } else { LD(((n) + WIN) & 7, rn, nxt); } FENCE(); } while (0)
#define PACE(sprint) do { asm volatile("" : "+s"(now), "+v"(ref)); \
      const int late = (int)(((unsigned)now - (unsigned)__builtin_amdgcn_readfirstlane(ref)) * 64u - due); \
      due += (TE - TS) * 64u / (2 * NT); \
      boost = (sprint) ? 14 + (late >> 7) : late >> 8; FENCE(); } while (0)

    float m = -INFINITY;
    float* sw = lds + L_S + wave * (NT * 64) + r * 16 + (c & 8) + 2 * (c & 2) + ((c >> 1) & 2) + (c & 1);
#pragma unroll 1
    for (int it = 1; it <= NT; ++it) {
      u64 now = __builtin_amdgcn_s_memrealtime();
      unsigned ref = lds_u[L_REF];
      const bool more = it < NT;
      const unsigned nxt = more ? cur + 2 * PIECE : first;
      const auto rc = rk;
      const auto rn = __builtin_amdgcn_make_buffer_rsrc((void*)(more ? kb : vb), 0, HEAD_BYTES, RSRC);
      float y[8];
#define QKSTEP(i) { REFILL(i); const u32x4 kk = B[i]; float a = 0.f, b = 0.f; \
      _Pragma("unroll") for (int k = 0; k < 4; ++k) { a = dot2(kk[k], qa[k], a); b = dot2(kk[k], qb[k], b); } \
      y[i] = a + dpp<XOR1>(b); asm volatile("" : "+v"(y[i])); FENCE(); }
      QKSTEP(0) QKSTEP(1) QKSTEP(2) QKSTEP(3)
      PACE(false);
      if (it <= CALM) boost = 0;
      QKSTEP(4) QKSTEP(5) QKSTEP(6) QKSTEP(7)
      float z[4], u0, u1, sc;
#pragma unroll
      for (int i = 0; i < 4; ++i) { const float t1 = y[i] + dpp<XOR8>(y[i]), t2 = y[i + 4] + dpp<XOR8>(y[i + 4]); z[i] = (c & 8) ? t2 : t1; }
      { const float t1 = z[0] + dpp<XOR2>(z[0]), t2 = z[2] + dpp<XOR2>(z[2]); u0 = (c & 2) ? t2 : t1; }
      { const float t1 = z[1] + dpp<XOR2>(z[1]), t2 = z[3] + dpp<XOR2>(z[3]); u1 = (c & 2) ? t2 : t1; }
      { const float t1 = u0 + dpp<SHL4>(u0), t2 = u1 + dpp<SHR4>(u1); sc = (c & 4) ? t2 : t1; }
      sc *= SCALE * LOG2E;
      m = fmaxf(m, sc);
      *sw = sc; sw += 64;
      cur = nxt;
    }
    due += TURN * 64u;
    m = fmaxf(m, dpp<XOR2>(m)); m = fmaxf(m, dpp<ROR4>(m)); m = fmaxf(m, dpp<XOR8>(m)); m = xrow_max(m);
    const f32x2 mneg = {-dpp<BCAST + 0>(m), -dpp<BCAST + 1>(m)};

    f32x2 acc[8], lsum = {0.f, 0.f};
#pragma unroll
    for (int k = 0; k < 8; ++k) acc[k] = f32x2{0.f, 0.f};
    const f32x4* sr = reinterpret_cast<const f32x4*>(lds + L_S + wave * (NT * 64) + r * 16);
    const auto rv = __builtin_amdgcn_make_buffer_rsrc((void*)vb, 0, HEAD_BYTES, RSRC);
#pragma unroll 1
    for (int it = 1; it <= NT; ++it) {
      u64 now = __builtin_amdgcn_s_memrealtime();
      unsigned ref = lds_u[L_REF];
      const f32x4 s4[4] = {sr[0], sr[1], sr[2], sr[3]};
      sr += 16;
      const bool more = it < NT;
      const unsigned nxt = more ? cur + 2 * PIECE : 0u;
      const auto rc = rv;
      const auto rn = __builtin_amdgcn_make_buffer_rsrc((void*)vb, 0, more ? HEAD_BYTES : 0u, RSRC);
#define PVSTEP(i) { REFILL(i); const f32x2 e = f32x2{s4[(i) >> 1][2 * ((i) & 1)], s4[(i) >> 1][2 * ((i) & 1) + 1]} + mneg; \
      f32x2 pp = {__builtin_amdgcn_exp2f(e[0]), __builtin_amdgcn_exp2f(e[1])}; lsum += pp; \
      asm volatile("" : "+v"(pp), "+v"(lsum)); FENCE(); \
      const u32x4 vv = B[i]; \
      _Pragma("unroll") for (int k = 0; k < 4; ++k) { const float lo = bflo(vv[k]), hi = bfhi(vv[k]); \
        acc[2 * k] = __builtin_elementwise_fma(pp, f32x2{lo, lo}, acc[2 * k]); \
        acc[2 * k + 1] = __builtin_elementwise_fma(pp, f32x2{hi, hi}, acc[2 * k + 1]); } \
      asm volatile("" : "+v"(acc[0]), "+v"(acc[1]), "+v"(acc[2]), "+v"(acc[3]), "+v"(acc[4]), "+v"(acc[5]), "+v"(acc[6]), "+v"(acc[7])); \
      FENCE(); }
      PVSTEP(0) PVSTEP(1) PVSTEP(2) PVSTEP(3)
      PACE(it + SPRINT > NT);
      PVSTEP(4) PVSTEP(5) PVSTEP(6) PVSTEP(7)
      cur = nxt;
    }

    float a0[8], a1[8];
#pragma unroll
    for (int k = 0; k < 8; ++k) { a0[k] = acc[k][0]; a1[k] = acc[k][1]; }
    float l = rs32(lsum[0], lsum[1]); l = rs16(l, l);
    asm volatile("s_nop 1\n\tv_permlane32_swap_b32 %0, %8\n\tv_permlane32_swap_b32 %1, %9\n\tv_permlane32_swap_b32 %2, %10\n\tv_permlane32_swap_b32 %3, %11\n\t"
                 "v_permlane32_swap_b32 %4, %12\n\tv_permlane32_swap_b32 %5, %13\n\tv_permlane32_swap_b32 %6, %14\n\tv_permlane32_swap_b32 %7, %15\n\t"
                 "v_add_f32 %0, %0, %8\n\tv_add_f32 %4, %4, %12\n\tv_add_f32 %1, %1, %9\n\tv_add_f32 %5, %5, %13\n\t"
                 "v_add_f32 %2, %2, %10\n\tv_add_f32 %6, %6, %14\n\tv_add_f32 %3, %3, %11\n\tv_add_f32 %7, %7, %15\n\t"
                 "v_permlane16_swap_b32 %0, %4\n\tv_permlane16_swap_b32 %1, %5\n\tv_permlane16_swap_b32 %2, %6\n\tv_permlane16_swap_b32 %3, %7"
                 : "+v"(a0[0]), "+v"(a0[1]), "+v"(a0[2]), "+v"(a0[3]), "+v"(a0[4]), "+v"(a0[5]), "+v"(a0[6]), "+v"(a0[7]),
                   "+v"(a1[0]), "+v"(a1[1]), "+v"(a1[2]), "+v"(a1[3]), "+v"(a1[4]), "+v"(a1[5]), "+v"(a1[6]), "+v"(a1[7]));
    reinterpret_cast<f32x4*>(lds)[oaddr] = f32x4{a0[0] + a0[4], a0[1] + a0[5], a0[2] + a0[6], a0[3] + a0[7]};
    if ((lane & 31) == (lane >> 5)) { lds[L_ML + (lane >> 5) * 2 * NW + wave] = m; lds[L_ML + (lane >> 5) * 2 * NW + NW + wave] = l; }
    __syncthreads();
    const f32x4 mm = *reinterpret_cast<const f32x4*>(lds + L_ML + (tid >> 7) * 2 * NW);
    const f32x4 ll = *reinterpret_cast<const f32x4*>(lds + L_ML + (tid >> 7) * 2 * NW + NW);
    float oo[NW];
#pragma unroll
    for (int w = 0; w < NW; ++w) oo[w] = lds[L_O + w * 2 * D + tid];
    const unsigned tag = lds_u[L_TAG + (tid >> 4)];
    FENCE();
    const float M = fmaxf(fmaxf(mm[0], mm[1]), fmaxf(mm[2], mm[3]));
    float L = 0.f, o = 0.f;
#pragma unroll
    for (int w = 0; w < NW; ++w) { const float wt = __builtin_amdgcn_exp2f(mm[w] - M); L = fmaf(wt, ll[w], L); o = fmaf(wt, oo[w], o); }
    const unsigned ov = (__float_as_uint(o * __builtin_amdgcn_rcpf(L)) & ~31u) | tag;
    __hip_atomic_store(inbox + (unsigned)paddr, pack(ov, __float_as_uint(M + __builtin_amdgcn_logf(L))), RLX_AGENT);
  }
}

extern "C" void kernel_launch(void* const* d_in, const int* in_sizes, int n_in,
                              void* d_out, int out_size, void* d_ws, size_t ws_size,
                              hipStream_t stream) {
  attn_fused<<<HKV * NSPLIT, NTHR, 0, stream>>>((const unsigned short*)d_in[0], (const unsigned short*)d_in[1], (const unsigned short*)d_in[2],
                                               (const unsigned short*)d_in[3], (const unsigned short*)d_in[4], (const unsigned short*)d_in[5],
                                               (unsigned short*)d_out, (u64*)d_ws);
}
```
